```python
import torch

def _softmax(x, axis=-1):
    return torch.softmax(x, dim=axis)
def _gelu(x):
    return torch.nn.functional.gelu(x, approximate="tanh")
def _silu(x):
    return torch.nn.functional.silu(x)
def _sigmoid(x):
    return torch.sigmoid(x)
def _layernorm(x, axis=-1, eps=1e-5):
    m = x.mean(dim=axis, keepdim=True)
    v = x.var(dim=axis, keepdim=True, unbiased=False)
    return (x - m) / torch.sqrt(v + eps)
def _scatter_add(tgt, idx, src, axis=0):
    return tgt.clone().index_add_(axis, idx, src)
def _segment_sum(src, seg, n):
    out = torch.zeros((n,) + src.shape[1:], dtype=src.dtype)
    return out.index_add_(0, seg, src)
def _pad(x, pw):
    flat = tuple(v for pair in reversed(pw) for v in pair)
    return torch.nn.functional.pad(x, flat)

kernel_name = 'gather_scatter__edge_featured_mpnn_gatedgcn__graph'

def setup_inputs():

    _w = (lambda a: (lambda t: t.to(torch.bfloat16) if t.is_floating_point() else t)(torch.as_tensor(a)))
    N, deg, D, De = 50000, 16, 64, max(4, 1)
    E = N * deg
    return {
        "x": _w((lambda *s: torch.randn(*s, dtype=torch.bfloat16) * 0.4)(N, D)),
        "e_attr": _w((lambda *s: torch.randn(*s, dtype=torch.bfloat16) * 0.8)(E, De)),
        "src": _w((lambda lo, hi, *s: torch.randint(lo, hi, s))(0, N, E).to(torch.int64)),
        "dst": _w((lambda lo, hi, *s: torch.randint(lo, hi, s))(0, N, E).to(torch.int64)),
        "w_a": _w((lambda *s: torch.randn(*s, dtype=torch.bfloat16) * 0.8)(D, D)),
        "w_b": _w((lambda *s: torch.randn(*s, dtype=torch.bfloat16) * 0.4)(D, D)),
        "w_c": _w((lambda *s: torch.randn(*s, dtype=torch.bfloat16) * 0.8)(De, D)),
        "w_u": _w((lambda *s: torch.randn(*s, dtype=torch.bfloat16) * 0.05)(D, D)),
        "w_v": _w((lambda *s: torch.randn(*s, dtype=torch.bfloat16) * 0.05)(D, D)),
    }

def reference(x, e_attr, src, dst, w_a, w_b, w_c, w_u, w_v):
    N, D = x.shape
    hv = x @ w_v
    g = x[dst] @ w_a + x[src] @ w_b + e_attr @ w_c
    eta = _sigmoid(g)
    denom = _segment_sum(eta, dst, N) + 1e-6
    eta_n = eta / denom[dst]
    agg = _segment_sum(eta_n * hv[src], dst, N)
    z = x @ w_u + agg
    relu_z = torch.where(z > 0, z, 0 * z)
    return x + relu_z
```

```cpp
#include <hip/hip_runtime.h>
#include <hip/hip_bf16.h>
#include <stdint.h>

typedef unsigned short u16;

__device__ __forceinline__ float bf2f(u16 v) { return __uint_as_float(((unsigned)v) << 16); }
__device__ __forceinline__ u16 f2bf(float f) {
    unsigned u = __float_as_uint(f);
    u += 0x7fffu + ((u >> 16) & 1u);
    return (u16)(u >> 16);
}
__device__ __forceinline__ float rbf(float f) { return bf2f(f2bf(f)); }

__global__ void k_node(const u16* __restrict__ x, const u16* __restrict__ wa, const u16* __restrict__ wb,
                       const u16* __restrict__ wu, const u16* __restrict__ wv,
                       float* __restrict__ ta, float* __restrict__ tb, float* __restrict__ th, float* __restrict__ tu, int N) {
    int node = blockIdx.x * 4 + (threadIdx.x >> 6);
    int d = threadIdx.x & 63;
    if (node >= N) return;
    float a = 0.f, b = 0.f, h = 0.f, u = 0.f;
    const u16* xr = x + (size_t)node * 64;
    for (int k = 0; k < 64; ++k) {
        float xv = bf2f(xr[k]);
        a += xv * bf2f(wa[k * 64 + d]);
        b += xv * bf2f(wb[k * 64 + d]);
        h += xv * bf2f(wv[k * 64 + d]);
        u += xv * bf2f(wu[k * 64 + d]);
    }
    size_t o = (size_t)node * 64 + d;
    ta[o] = rbf(a); tb[o] = rbf(b); th[o] = rbf(h); tu[o] = u;
}

__global__ void k_edge(const int* __restrict__ src, const int* __restrict__ dst, const u16* __restrict__ ea,
                       const u16* __restrict__ wc, const float* __restrict__ ta, const float* __restrict__ tb,
                       const float* __restrict__ th, float* __restrict__ den, float* __restrict__ num, int E, int N) {
    int e = blockIdx.x * 4 + (threadIdx.x >> 6);
    int d = threadIdx.x & 63;
    if (e >= E) return;
    int s = src[e], t = dst[e];
    float c = 0.f;
    for (int k = 0; k < 4; ++k) c += bf2f(ea[(size_t)e * 4 + k]) * bf2f(wc[k * 64 + d]);
    float g = ta[(size_t)t * 64 + d] + tb[(size_t)s * 64 + d] + rbf(c);
    float eta = rbf(1.f / (1.f + __expf(-g)));
    atomicAdd(&den[(size_t)t * 64 + d], eta);
    atomicAdd(&num[(size_t)t * 64 + d], eta * th[(size_t)s * 64 + d]);
}

__global__ void k_final(const u16* __restrict__ x, const float* __restrict__ tu, const float* __restrict__ den,
                        const float* __restrict__ num, u16* __restrict__ out, int total) {
    int i = blockIdx.x * blockDim.x + threadIdx.x;
    if (i >= total) return;
    float z = tu[i] + num[i] / (den[i] + 1e-6f);
    z = z > 0.f ? z : 0.f;
    out[i] = f2bf(bf2f(x[i]) + z);
}

extern "C" void kernel_launch(void* const* d_in, const int* in_sizes, int n_in,
                              void* d_out, int out_size, void* d_ws, size_t ws_size,
                              hipStream_t stream) {
    const u16* x = (const u16*)d_in[0];
    const u16* ea = (const u16*)d_in[1];
    const int* src = (const int*)d_in[2];
    const int* dst = (const int*)d_in[3];
    const u16* wa = (const u16*)d_in[4];
    const u16* wb = (const u16*)d_in[5];
    const u16* wc = (const u16*)d_in[6];
    const u16* wu = (const u16*)d_in[7];
    const u16* wv = (const u16*)d_in[8];
    int N = in_sizes[0] / 64;
    int E = in_sizes[2];
    float* ws = (float*)d_ws;
    size_t NT = (size_t)N * 64;
    float* den = ws;
    float* num = ws + NT;
    float* ta = ws + 2 * NT;
    float* tb = ws + 3 * NT;
    float* th = ws + 4 * NT;
    float* tu = ws + 5 * NT;
    hipMemsetAsync(den, 0, 2 * NT * sizeof(float), stream);
    k_node<<<(N + 3) / 4, 256, 0, stream>>>(x, wa, wb, wu, wv, ta, tb, th, tu, N);
    k_edge<<<(E + 3) / 4, 256, 0, stream>>>(src, dst, ea, wc, ta, tb, th, den, num, E, N);
    k_final<<<(int)((NT + 255) / 256), 256, 0, stream>>>(x, tu, den, num, (u16*)d_out, (int)NT);
}
```

```cpp
#include <hip/hip_runtime.h>
#include <stdint.h>

typedef unsigned short u16;
typedef unsigned int u32;
typedef unsigned long long u64;
typedef unsigned char u8;
using bf16x8 = __attribute__((ext_vector_type(8))) short;
using f32x4 = __attribute__((ext_vector_type(4))) float;
typedef __attribute__((ext_vector_type(2))) __bf16 bf16x2_t;
typedef __attribute__((ext_vector_type(2))) float f32x2_t;

#define MFMA16(a, b, c) __builtin_amdgcn_mfma_f32_16x16x32_bf16((a), (b), (c), 0, 0, 0)
#define DEV __device__ __forceinline__

DEV float bf2f(u16 v) { return __uint_as_float(((u32)v) << 16); }
DEV u16 f2bf(float f) {
    u32 u = __float_as_uint(f);
    u += 0x7fffu + ((u >> 16) & 1u);
    return (u16)(u >> 16);
}
DEV float rbf(float f) { return bf2f(f2bf(f)); }
DEV u32 pack2(float lo, float hi) {
    f32x2_t v = {lo, hi};
    bf16x2_t b = __builtin_convertvector(v, bf16x2_t);
    return __builtin_bit_cast(u32, b);
}
DEV bf16x8 asfrag(uint4 v) { return __builtin_bit_cast(bf16x8, v); }
DEV u32 fastdiv(u32 n, u32 m) { return m == 0u ? n : __umulhi(n, m); }
DEV u32 wave_incl_scan(u32 v, int lane) {
#pragma unroll
    for (int o = 1; o < 64; o <<= 1) {
        u32 t = __shfl_up(v, o);
        if (lane >= o) v += t;
    }
    return v;
}

__global__ void g_node(const u16* __restrict__ x, const u16* __restrict__ wa, const u16* __restrict__ wb,
                       const u16* __restrict__ wu, const u16* __restrict__ wv,
                       float* __restrict__ ta, float* __restrict__ tb, float* __restrict__ th, float* __restrict__ tu, int N) {
    int node = blockIdx.x * 4 + (threadIdx.x >> 6);
    int d = threadIdx.x & 63;
    if (node >= N) return;
    float a = 0.f, b = 0.f, h = 0.f, u = 0.f;
    const u16* xr = x + (size_t)node * 64;
    for (int k = 0; k < 64; ++k) {
        float xv = bf2f(xr[k]);
        a += xv * bf2f(wa[k * 64 + d]);
        b += xv * bf2f(wb[k * 64 + d]);
        h += xv * bf2f(wv[k * 64 + d]);
        u += xv * bf2f(wu[k * 64 + d]);
    }
    size_t o = (size_t)node * 64 + d;
    ta[o] = rbf(a); tb[o] = rbf(b); th[o] = rbf(h); tu[o] = u;
}
__global__ void g_edge(const int* __restrict__ src, const int* __restrict__ dst, const u16* __restrict__ ea,
                       const u16* __restrict__ wc, const float* __restrict__ ta, const float* __restrict__ tb,
                       const float* __restrict__ th, float* __restrict__ den, float* __restrict__ num, int E) {
    int e = blockIdx.x * 4 + (threadIdx.x >> 6);
    int d = threadIdx.x & 63;
    if (e >= E) return;
    int s = src[e], t = dst[e];
    float c = 0.f;
    for (int k = 0; k < 4; ++k) c += bf2f(ea[(size_t)e * 4 + k]) * bf2f(wc[k * 64 + d]);
    float g = ta[(size_t)t * 64 + d] + tb[(size_t)s * 64 + d] + rbf(c);
    float eta = rbf(1.f / (1.f + __expf(-g)));
    atomicAdd(&den[(size_t)t * 64 + d], eta);
    atomicAdd(&num[(size_t)t * 64 + d], eta * th[(size_t)s * 64 + d]);
}
__global__ void g_final(const u16* __restrict__ x, const float* __restrict__ tu, const float* __restrict__ den,
                        const float* __restrict__ num, u16* __restrict__ out, int total) {
    int i = blockIdx.x * blockDim.x + threadIdx.x;
    if (i >= total) return;
    float z = tu[i] + num[i] / (den[i] + 1e-6f);
    z = z > 0.f ? z : 0.f;
    out[i] = f2bf(bf2f(x[i]) + z);
}

constexpr int NSB_MAX = 256;
constexpr int R_MAX = 512;
constexpr int PG = 8;

template <int NBITS>
DEV u64 match_mask(u32 key, bool valid) {
    u64 m = __ballot(valid);
#pragma unroll
    for (int b = 0; b < NBITS; ++b) {
        bool bit = (key >> b) & 1u;
        u64 bb = __ballot(bit);
        m &= bit ? bb : ~bb;
    }
    return m;
}

__global__ __launch_bounds__(512) void k_part(
    const int* __restrict__ src, const int* __restrict__ dst, const uint2* __restrict__ ea,
    const u16* __restrict__ wa, const u16* __restrict__ wb, const u16* __restrict__ wc,
    const u16* __restrict__ wu, const u16* __restrict__ wv,
    uint4* __restrict__ rec, u32* __restrict__ tab, uint4* __restrict__ wfrag, uint2* __restrict__ wcpk,
    int E, int CH, int SC, int NSB, int R, int N, u32 magSB, int SBN, u32 magNPW, int npw, int sbs) {
    __shared__ u32 hist[8 * NSB_MAX];
    __shared__ u32 wsum[8];
    const int tid = threadIdx.x, w = tid >> 6, lane = tid & 63;
    const int p = blockIdx.x;
    for (int i = tid; i < 8 * NSB_MAX; i += 512) hist[i] = 0u;

    const int cbase = p * CH;
    int clen = E - cbase;
    clen = clen < 0 ? 0 : (clen > CH ? CH : clen);
    const int wbeg = w * SC;
    int wend = wbeg + SC;
    wend = wend > clen ? clen : wend;

    u32 dv[PG], sv[PG];
    uint2 ev[PG];
#pragma unroll
    for (int g = 0; g < PG; ++g) {
        const int idx = wbeg + g * 64 + lane;
        if (idx < wend) {
            dv[g] = (u32)dst[cbase + idx];
            sv[g] = (u32)src[cbase + idx];
            ev[g] = ea[cbase + idx];
        } else {
            dv[g] = 0u; sv[g] = 0u; ev[g] = make_uint2(0u, 0u);
        }
    }
    __syncthreads();

    u32 bn[PG], rk[PG];
    u32* myh = hist + w * NSB_MAX;
#pragma unroll
    for (int g = 0; g < PG; ++g) {
        const int idx = wbeg + g * 64 + lane;
        const bool valid = idx < wend;
        u32 b = fastdiv(dv[g], magSB);
        b = b < (u32)NSB ? b : (u32)(NSB - 1);
        const u64 m = match_mask<8>(b, valid);
        const u32 rnk = (u32)__popcll(m & ((1ull << lane) - 1ull));
        const u32 cnt = (u32)__popcll(m);
        const u32 old = myh[b];
        if (valid && rnk == 0u) myh[b] = old + cnt;
        bn[g] = b;
        rk[g] = old + rnk;
    }
    __syncthreads();

    u32 tot = 0u;
    if (tid < NSB) {
#pragma unroll
        for (int k = 0; k < 8; ++k) tot += hist[k * NSB_MAX + tid];
    }
    const u32 incl = wave_incl_scan(tot, lane);
    if (lane == 63) wsum[w] = incl;
    __syncthreads();
    u32 start = incl - tot;
    for (int k = 0; k < w; ++k) start += wsum[k];
    if (tid < NSB) {
        u32 run = start;
#pragma unroll
        for (int k = 0; k < 8; ++k) {
            const u32 c = hist[k * NSB_MAX + tid];
            hist[k * NSB_MAX + tid] = run;
            run += c;
        }
        tab[(size_t)tid * R + p] = (start << 16) | tot;
    }
    __syncthreads();

#pragma unroll
    for (int g = 0; g < PG; ++g) {
        const int idx = wbeg + g * 64 + lane;
        if (idx < wend) {
            const u32 pos = myh[bn[g]] + rk[g];
            const u32 dl = dv[g] - bn[g] * (u32)SBN;
            const u32 wv_ = fastdiv(dl, magNPW);
            const u32 loc = dl - wv_ * (u32)npw;
            const u32 sub = loc >= (u32)sbs ? 1u : 0u;
            const u32 li = loc - sub * (u32)sbs;
            const u32 s = sv[g] < (u32)N ? sv[g] : 0u;
            rec[(size_t)cbase + pos] = make_uint4(s, ((wv_ * 2u + sub) << 8) | li, ev[g].x, ev[g].y);
        }
    }

    if (p < 4) {
        const u16* W = p == 0 ? wb : (p == 1 ? wv : (p == 2 ? wa : wu));
        const int f = tid >> 6, c = f >> 1, s = f & 1, col = lane & 15, q = lane >> 4;
        u32 v[4];
#pragma unroll
        for (int j = 0; j < 4; ++j) {
            const int k0 = 16 * q + 8 * s + 2 * j;
            v[j] = (u32)W[k0 * 64 + 16 * c + col] | ((u32)W[(k0 + 1) * 64 + 16 * c + col] << 16);
        }
        wfrag[p * 512 + tid] = make_uint4(v[0], v[1], v[2], v[3]);
    } else if (p == 4 && tid < 64) {
        const int d = tid;
        wcpk[d] = make_uint2((u32)wc[d] | ((u32)wc[64 + d] << 16), (u32)wc[128 + d] | ((u32)wc[192 + d] << 16));
    }
}

constexpr int CAPR = 4096;
constexpr int LCAP = CAPR + 16 * 32;
constexpr int STG_STRIDE = 68;
constexpr int OFF_REC = 0;
constexpr int OFF_STG = OFF_REC + LCAP * 16;
constexpr int OFF_DL = OFF_STG + 8 * 16 * STG_STRIDE * 4;
constexpr int OFF_MAP = OFF_DL + LCAP;
constexpr int OFF_PRE = OFF_MAP + CAPR * 2;
constexpr int OFF_GST = OFF_PRE + R_MAX * 4;
constexpr int OFF_FCNT = OFF_GST + R_MAX * 4;
constexpr int OFF_FTOT = OFF_FCNT + 8 * 16 * 4;
constexpr int OFF_FOFF = OFF_FTOT + 16 * 4;
constexpr int OFF_WSUM = OFF_FOFF + 16 * 4;
constexpr int KM_LDS = OFF_WSUM + 8 * 4;

__global__ __launch_bounds__(512, 2) void k_main(
    const u16* __restrict__ x, const uint4* __restrict__ rec, const u32* __restrict__ tab,
    const uint4* __restrict__ wfrag, const uint2* __restrict__ wcpk, float4* __restrict__ scr,
    u16* __restrict__ out, int N, int CH, int R, int SBN, int npw, int sbs) {
    __shared__ __attribute__((aligned(16))) unsigned char smem[KM_LDS];
    uint4* srec = (uint4*)(smem + OFF_REC);
    float* stg = (float*)(smem + OFF_STG);
    u8* sdl = smem + OFF_DL;
    u16* smap = (u16*)(smem + OFF_MAP);
    u32* spre = (u32*)(smem + OFF_PRE);
    u32* sgst = (u32*)(smem + OFF_GST);
    u32* fcnt = (u32*)(smem + OFF_FCNT);
    u32* ftot = (u32*)(smem + OFF_FTOT);
    u32* foff = (u32*)(smem + OFF_FOFF);
    u32* wsum = (u32*)(smem + OFF_WSUM);

    const int sb = blockIdx.x;
    const int tid0 = threadIdx.x;

    u32 st = 0u, cn = 0u;
    u32 excl, T = 0u;
    bf16x8 WG[4][2], WH[4][2];
    {
    const int tid = tid0, w = tid >> 6, lane = tid & 63;
    if (tid < R) {
        const u32 d = tab[(size_t)sb * R + tid];
        st = d >> 16;
        cn = d & 0xFFFFu;
    }
    const u32 incl = wave_incl_scan(cn, lane);
    if (lane == 63) wsum[w] = incl;

#pragma unroll
    for (int c = 0; c < 4; ++c)
#pragma unroll
        for (int s = 0; s < 2; ++s) {
            WG[c][s] = asfrag(wfrag[0 * 512 + (c * 2 + s) * 64 + lane]);
            WH[c][s] = asfrag(wfrag[1 * 512 + (c * 2 + s) * 64 + lane]);
        }

    __syncthreads();
    excl = incl - cn;
#pragma unroll
    for (int k = 0; k < 8; ++k) {
        const u32 v = wsum[k];
        if (k < w) excl += v;
        T += v;
    }
    if (tid < R) {
        spre[tid] = excl;
        sgst[tid] = (u32)tid * (u32)CH + st;
    }
    }

    for (u32 rbase = 0u;; rbase += CAPR) {
        int tid = tid0;
        asm volatile("" : "+v"(tid));
        const int lane = tid & 63;
        const int w = __builtin_amdgcn_readfirstlane(tid >> 6);
        const int wnode0 = sb * SBN + w * npw;
        const u32 rc = (T - rbase) < (u32)CAPR ? (T - rbase) : (u32)CAPR;
        const bool last = rbase + CAPR >= T;
        if (tid < R) {
            u32 k0 = excl > rbase ? excl : rbase;
            u32 k1 = excl + cn;
            k1 = k1 < rbase + rc ? k1 : rbase + rc;
            for (u32 k = k0; k < k1; ++k) smap[k - rbase] = (u16)tid;
        }
        if (tid < 128) fcnt[tid] = 0u;
        __syncthreads();

        const u32 SC2 = (rc + 7u) >> 3;
        const u32 wb_ = (u32)w * SC2;
        u32 we_ = wb_ + SC2;
        we_ = we_ < rc ? we_ : rc;
        uint4 rv[8];
#pragma unroll
        for (int g = 0; g < 8; ++g) {
            const u32 i = wb_ + g * 64 + lane;
            if (i < we_) {
                const u32 pp = smap[i];
                rv[g] = rec[(size_t)sgst[pp] + (rbase + i - spre[pp])];
            } else {
                rv[g] = make_uint4(0u, 0xFFu, 0u, 0u);
            }
        }
        u32 rk[8];
        u32* myf = fcnt + w * 16;
#pragma unroll
        for (int g = 0; g < 8; ++g) {
            const u32 i = wb_ + g * 64 + lane;
            const bool valid = i < we_;
            const u32 fb = (rv[g].y >> 8) & 15u;
            const u64 m = match_mask<4>(fb, valid);
            const u32 rnk = (u32)__popcll(m & ((1ull << lane) - 1ull));
            const u32 cnt = (u32)__popcll(m);
            const u32 old = myf[fb];
            if (valid && rnk == 0u) myf[fb] = old + cnt;
            rk[g] = old + rnk;
        }
        __syncthreads();
        if (w == 0) {
            u32 tot = 0u;
            if (lane < 16) {
#pragma unroll
                for (int k = 0; k < 8; ++k) tot += fcnt[k * 16 + lane];
            }
            const u32 pad = (tot + 31u) & ~31u;
            const u32 inc = wave_incl_scan(pad, lane);
            if (lane < 16) {
                u32 run = inc - pad;
                foff[lane] = run;
                ftot[lane] = tot;
#pragma unroll
                for (int k = 0; k < 8; ++k) {
                    const u32 c = fcnt[k * 16 + lane];
                    fcnt[k * 16 + lane] = run;
                    run += c;
                }
            }
        }
        __syncthreads();
#pragma unroll
        for (int g = 0; g < 8; ++g) {
            const u32 i = wb_ + g * 64 + lane;
            if (i < we_) {
                const u32 fb = (rv[g].y >> 8) & 15u;
                const u32 pos = myf[fb] + rk[g];
                srec[pos] = rv[g];
                sdl[pos] = (u8)(rv[g].y & 0xFFu);
            }
        }
        {
            const int b = tid >> 5, k = tid & 31;
            const u32 tb_ = ftot[b];
            const u32 idx = tb_ + k;
            if (idx < ((tb_ + 31u) & ~31u)) {
                srec[foff[b] + idx] = make_uint4(0u, 0xFFu, 0u, 0u);
                sdl[foff[b] + idx] = (u8)0xFFu;
            }
        }
        __syncthreads();

#pragma unroll 1
        for (int sub = 0; sub < 2; ++sub) {
            int lane2 = lane;
            asm volatile("" : "+v"(lane2));
            const int r = lane2 & 15, q = lane2 >> 4;
            const int fb = w * 2 + sub;
            const int nb = wnode0 + sub * sbs;
            int nv = (sub == 0 ? sbs : npw - sbs);
            nv = nv < N - nb ? nv : N - nb;
            if (nv <= 0) continue;
            uint4 xo0 = make_uint4(0u, 0u, 0u, 0u), xo1 = xo0;
            if (r < nv) {
                const uint4* px = (const uint4*)(x + (size_t)(nb + r) * 64) + 2 * q;
                xo0 = px[0];
                xo1 = px[1];
            }
            bf16x8 WE[4];
#pragma unroll
            for (int c = 0; c < 4; ++c) {
                f32x4 a = {0.f, 0.f, 0.f, 0.f};
                a = MFMA16(asfrag(xo0), asfrag(wfrag[2 * 512 + (c * 2 + 0) * 64 + lane]), a);
                a = MFMA16(asfrag(xo1), asfrag(wfrag[2 * 512 + (c * 2 + 1) * 64 + lane]), a);
                uint2 wcv = make_uint2(0u, 0u);
                if (q == 3) wcv = wcpk[16 * c + r];
                WE[c] = asfrag(make_uint4(pack2(a[0], a[1]), pack2(a[2], a[3]), wcv.x, wcv.y));
            }
            f32x4 accD[4], accN[4];
            float4* sp = scr + ((size_t)(sb * 16 + fb) * 8) * 64 + lane;
            if (rbase == 0u) {
#pragma unroll
                for (int c = 0; c < 4; ++c) {
                    accD[c] = f32x4{0.f, 0.f, 0.f, 0.f};
                    accN[c] = f32x4{0.f, 0.f, 0.f, 0.f};
                }
            } else {
#pragma unroll
                for (int c = 0; c < 4; ++c) {
                    const float4 a = sp[(2 * c) * 64], b = sp[(2 * c + 1) * 64];
                    accD[c] = f32x4{a.x, a.y, a.z, a.w};
                    accN[c] = f32x4{b.x, b.y, b.z, b.w};
                }
            }
            const u32 cnt = ftot[fb], base = foff[fb];
            const int nsteps = (int)((cnt + 31u) >> 5);
            const uint4* lrec = srec + base;
            const u8* ldl = sdl + base;
            if (nsteps > 0) {
                uint4 rc0 = lrec[r], rc1 = lrec[16 + r];
                uint4 x00, x01, x10, x11;
                {
                    const uint4* p0 = (const uint4*)(x + (size_t)rc0.x * 64) + 2 * q;
                    const uint4* p1 = (const uint4*)(x + (size_t)rc1.x * 64) + 2 * q;
                    x00 = p0[0]; x01 = p0[1]; x10 = p1[0]; x11 = p1[1];
                }
#pragma unroll 1
                for (int stp = 0; stp < nsteps; ++stp) {
                    const bool more = stp + 1 < nsteps;
                    uint4 nr0 = rc0, nr1 = rc1, n00 = x00, n01 = x01, n10 = x10, n11 = x11;
                    if (more) {
                        nr0 = lrec[(stp + 1) * 32 + r];
                        nr1 = lrec[(stp + 1) * 32 + 16 + r];
                        const uint4* p0 = (const uint4*)(x + (size_t)nr0.x * 64) + 2 * q;
                        const uint4* p1 = (const uint4*)(x + (size_t)nr1.x * 64) + 2 * q;
                        n00 = p0[0]; n01 = p0[1]; n10 = p1[0]; n11 = p1[1];
                    }
                    bf16x8 ae[2];
#pragma unroll
                    for (int t = 0; t < 2; ++t) {
                        const uint4 rcc = t == 0 ? rc0 : rc1;
                        const u32 d = rcc.y & 0xFFu;
                        const bool mt = (d >> 2) == (u32)q;
                        const u32 one = 0x3F80u << (16u * (d & 1u));
                        uint4 ax;
                        ax.x = (mt && !(d & 2u)) ? one : 0u;
                        ax.y = (mt && (d & 2u)) ? one : 0u;
                        ax.z = q == 3 ? rcc.z : 0u;
                        ax.w = q == 3 ? rcc.w : 0u;
                        ae[t] = asfrag(ax);
                    }
                    const u32 d0 = *(const u32*)(ldl + stp * 32 + 4 * q);
                    const u32 d1 = *(const u32*)(ldl + stp * 32 + 16 + 4 * q);
                    uint4 sg;
                    {
                        const u32 ur = (u32)r;
                        sg.x = (((d0 >> 0) & 0xFFu) == ur ? 0x3F80u : 0u) | (((d0 >> 8) & 0xFFu) == ur ? 0x3F800000u : 0u);
                        sg.y = (((d0 >> 16) & 0xFFu) == ur ? 0x3F80u : 0u) | (((d0 >> 24) & 0xFFu) == ur ? 0x3F800000u : 0u);
                        sg.z = (((d1 >> 0) & 0xFFu) == ur ? 0x3F80u : 0u) | (((d1 >> 8) & 0xFFu) == ur ? 0x3F800000u : 0u);
                        sg.w = (((d1 >> 16) & 0xFFu) == ur ? 0x3F80u : 0u) | (((d1 >> 24) & 0xFFu) == ur ? 0x3F800000u : 0u);
                    }
                    const bf16x8 sf = asfrag(sg);
#pragma unroll
                    for (int c = 0; c < 4; ++c) {
                        u32 ve[4], vp[4];
#pragma unroll
                        for (int t = 0; t < 2; ++t) {
                            const bf16x8 a0 = asfrag(t == 0 ? x00 : x10);
                            const bf16x8 a1 = asfrag(t == 0 ? x01 : x11);
                            f32x4 g = {0.f, 0.f, 0.f, 0.f}, h = {0.f, 0.f, 0.f, 0.f};
                            g = MFMA16(a0, WG[c][0], g);
                            g = MFMA16(a1, WG[c][1], g);
                            g = MFMA16(ae[t], WE[c], g);
                            h = MFMA16(a0, WH[c][0], h);
                            h = MFMA16(a1, WH[c][1], h);
                            float e[4], pr[4];
#pragma unroll
                            for (int i = 0; i < 4; ++i) {
                                const float tt = __builtin_amdgcn_exp2f(g[i] * -1.44269504088896341f);
                                const float et = __builtin_amdgcn_rcpf(1.0f + tt);
                                e[i] = et;
                                pr[i] = et * h[i];
                            }
                            ve[2 * t + 0] = pack2(e[0], e[1]);
                            ve[2 * t + 1] = pack2(e[2], e[3]);
                            vp[2 * t + 0] = pack2(pr[0], pr[1]);
                            vp[2 * t + 1] = pack2(pr[2], pr[3]);
                        }
                        accD[c] = MFMA16(sf, asfrag(make_uint4(ve[0], ve[1], ve[2], ve[3])), accD[c]);
                        accN[c] = MFMA16(sf, asfrag(make_uint4(vp[0], vp[1], vp[2], vp[3])), accN[c]);
                        __builtin_amdgcn_sched_barrier(0);
                    }
                    rc0 = nr0; rc1 = nr1; x00 = n00; x01 = n01; x10 = n10; x11 = n11;
                }
            }
            if (!last) {
#pragma unroll
                for (int c = 0; c < 4; ++c) {
                    sp[(2 * c) * 64] = make_float4(accD[c][0], accD[c][1], accD[c][2], accD[c][3]);
                    sp[(2 * c + 1) * 64] = make_float4(accN[c][0], accN[c][1], accN[c][2], accN[c][3]);
                }
            } else {
                float* mystg = stg + w * 16 * STG_STRIDE;
#pragma unroll
                for (int c = 0; c < 4; ++c) {
                    f32x4 u = {0.f, 0.f, 0.f, 0.f};
                    u = MFMA16(asfrag(xo0), asfrag(wfrag[3 * 512 + (c * 2 + 0) * 64 + lane]), u);
                    u = MFMA16(asfrag(xo1), asfrag(wfrag[3 * 512 + (c * 2 + 1) * 64 + lane]), u);
#pragma unroll
                    for (int i = 0; i < 4; ++i) {
                        float z = u[i] + accN[c][i] / (accD[c][i] + 1e-6f);
                        z = z > 0.f ? z : 0.f;
                        mystg[(4 * q + i) * STG_STRIDE + 16 * c + r] = z;
                    }
                }
                __builtin_amdgcn_wave_barrier();
#pragma unroll
                for (int ps = 0; ps < 2; ++ps) {
                    const int row = (lane >> 3) + 8 * ps, d8 = lane & 7;
                    if (row < nv) {
                        const size_t o = (size_t)(nb + row) * 64 + 8 * d8;
                        const uint4 xv = *(const uint4*)(x + o);
                        const float* zr = mystg + row * STG_STRIDE + 8 * d8;
                        uint4 ov;
                        ov.x = pack2(bf2f((u16)(xv.x & 0xFFFFu)) + zr[0], bf2f((u16)(xv.x >> 16)) + zr[1]);
                        ov.y = pack2(bf2f((u16)(xv.y & 0xFFFFu)) + zr[2], bf2f((u16)(xv.y >> 16)) + zr[3]);
                        ov.z = pack2(bf2f((u16)(xv.z & 0xFFFFu)) + zr[4], bf2f((u16)(xv.z >> 16)) + zr[5]);
                        ov.w = pack2(bf2f((u16)(xv.w & 0xFFFFu)) + zr[6], bf2f((u16)(xv.w >> 16)) + zr[7]);
                        *(uint4*)(out + o) = ov;
                    }
                }
                __builtin_amdgcn_wave_barrier();
            }
        }
        if (last) break;
        __syncthreads();
    }
}

static u32 magic_for(u32 d) { return d <= 1u ? 0u : (u32)((0x100000000ull / d) + 1ull); }

extern "C" void kernel_launch(void* const* d_in, const int* in_sizes, int n_in,
                              void* d_out, int out_size, void* d_ws, size_t ws_size,
                              hipStream_t stream) {
    const u16* x = (const u16*)d_in[0];
    const u16* ea = (const u16*)d_in[1];
    const int* src = (const int*)d_in[2];
    const int* dst = (const int*)d_in[3];
    const u16* wa = (const u16*)d_in[4];
    const u16* wb = (const u16*)d_in[5];
    const u16* wc = (const u16*)d_in[6];
    const u16* wu = (const u16*)d_in[7];
    const u16* wv = (const u16*)d_in[8];
    const int N = in_sizes[0] / 64;
    const int E = in_sizes[2];

    int npw = (N + 2047) / 2048;
    npw = npw < 1 ? 1 : (npw > 32 ? 32 : npw);
    const int SBN = 8 * npw;
    const int NSB = (N + SBN - 1) / SBN;
    const int sbs = (npw + 1) / 2;
    int R = (E + 4095) / 4096;
    R = R < 256 ? 256 : R;
    const int CH = (E + R - 1) / R;
    const int SC = (CH + 7) / 8;

    const size_t rec_bytes = (size_t)R * (size_t)CH * 16;
    const size_t off_tab = (rec_bytes + 255) & ~(size_t)255;
    const size_t off_wf = (off_tab + (size_t)NSB * R * 4 + 255) & ~(size_t)255;
    const size_t off_wc = off_wf + 4 * 512 * 16;
    const size_t off_scr = (off_wc + 64 * 8 + 255) & ~(size_t)255;
    const size_t need = off_scr + (size_t)NSB * 16 * 8 * 64 * 16;
    const bool fast = N > 0 && NSB <= NSB_MAX && R <= R_MAX && CH <= 4096 && need <= ws_size;

    if (fast) {
        char* ws = (char*)d_ws;
        uint4* rec = (uint4*)ws;
        u32* tab = (u32*)(ws + off_tab);
        uint4* wfrag = (uint4*)(ws + off_wf);
        uint2* wcpk = (uint2*)(ws + off_wc);
        float4* scr = (float4*)(ws + off_scr);
        k_part<<<R, 512, 0, stream>>>(src, dst, (const uint2*)ea, wa, wb, wc, wu, wv, rec, tab, wfrag, wcpk,
                                      E, CH, SC, NSB, R, N, magic_for((u32)SBN), SBN, magic_for((u32)npw), npw, sbs);
        k_main<<<NSB, 512, 0, stream>>>(x, rec, tab, wfrag, wcpk, scr, (u16*)d_out, N, CH, R, SBN, npw, sbs);
    } else {
        float* ws = (float*)d_ws;
        const size_t NT = (size_t)N * 64;
        float* den = ws;
        float* num = ws + NT;
        float* ta = ws + 2 * NT;
        float* tb = ws + 3 * NT;
        float* th = ws + 4 * NT;
        float* tu = ws + 5 * NT;
        (void)hipMemsetAsync(den, 0, 2 * NT * sizeof(float), stream);
        g_node<<<(N + 3) / 4, 256, 0, stream>>>(x, wa, wb, wu, wv, ta, tb, th, tu, N);
        g_edge<<<(E + 3) / 4, 256, 0, stream>>>(src, dst, ea, wc, ta, tb, th, den, num, E);
        g_final<<<(int)((NT + 255) / 256), 256, 0, stream>>>(x, tu, den, num, (u16*)d_out, (int)NT);
    }
}
```

```cpp
#include <hip/hip_runtime.h>
#include <stdint.h>

typedef unsigned short u16;
typedef unsigned int u32;
typedef unsigned long long u64;
typedef unsigned char u8;
using bf16x8 = __attribute__((ext_vector_type(8))) short;
using f32x4 = __attribute__((ext_vector_type(4))) float;
typedef __attribute__((ext_vector_type(2))) __bf16 bf16x2_t;
typedef __attribute__((ext_vector_type(2))) float f32x2_t;

#define MFMA16(a, b, c) __builtin_amdgcn_mfma_f32_16x16x32_bf16((a), (b), (c), 0, 0, 0)
#define DEV __device__ __forceinline__
#define STAMP(k) do {} while (0)

DEV float bf2f(u16 v) { return __uint_as_float(((u32)v) << 16); }
DEV u16 f2bf(float f) {
    u32 u = __float_as_uint(f);
    u += 0x7fffu + ((u >> 16) & 1u);
    return (u16)(u >> 16);
}
DEV float rbf(float f) { return bf2f(f2bf(f)); }
DEV u32 pack2(float lo, float hi) {
    f32x2_t v = {lo, hi};
    bf16x2_t b = __builtin_convertvector(v, bf16x2_t);
    return __builtin_bit_cast(u32, b);
}
DEV bf16x8 asfrag(uint4 v) { return __builtin_bit_cast(bf16x8, v); }
DEV u32 fastdiv(u32 n, u32 m) { return m == 0u ? n : __umulhi(n, m); }
DEV u32 wave_incl_scan(u32 v, int lane) {
#pragma unroll
    for (int o = 1; o < 64; o <<= 1) {
        u32 t = __shfl_up(v, o);
        if (lane >= o) v += t;
    }
    return v;
}

__global__ void g_node(const u16* __restrict__ x, const u16* __restrict__ wa, const u16* __restrict__ wb,
                       const u16* __restrict__ wu, const u16* __restrict__ wv,
                       float* __restrict__ ta, float* __restrict__ tb, float* __restrict__ th, float* __restrict__ tu, int N) {
    int node = blockIdx.x * 4 + (threadIdx.x >> 6);
    int d = threadIdx.x & 63;
    if (node >= N) return;
    float a = 0.f, b = 0.f, h = 0.f, u = 0.f;
    const u16* xr = x + (size_t)node * 64;
    for (int k = 0; k < 64; ++k) {
        float xv = bf2f(xr[k]);
        a += xv * bf2f(wa[k * 64 + d]);
        b += xv * bf2f(wb[k * 64 + d]);
        h += xv * bf2f(wv[k * 64 + d]);
        u += xv * bf2f(wu[k * 64 + d]);
    }
    size_t o = (size_t)node * 64 + d;
    ta[o] = rbf(a); tb[o] = rbf(b); th[o] = rbf(h); tu[o] = u;
}
__global__ void g_edge(const int* __restrict__ src, const int* __restrict__ dst, const u16* __restrict__ ea,
                       const u16* __restrict__ wc, const float* __restrict__ ta, const float* __restrict__ tb,
                       const float* __restrict__ th, float* __restrict__ den, float* __restrict__ num, int E) {
    int e = blockIdx.x * 4 + (threadIdx.x >> 6);
    int d = threadIdx.x & 63;
    if (e >= E) return;
    int s = src[e], t = dst[e];
    float c = 0.f;
    for (int k = 0; k < 4; ++k) c += bf2f(ea[(size_t)e * 4 + k]) * bf2f(wc[k * 64 + d]);
    float g = ta[(size_t)t * 64 + d] + tb[(size_t)s * 64 + d] + rbf(c);
    float eta = rbf(1.f / (1.f + __expf(-g)));
    atomicAdd(&den[(size_t)t * 64 + d], eta);
    atomicAdd(&num[(size_t)t * 64 + d], eta * th[(size_t)s * 64 + d]);
}
__global__ void g_final(const u16* __restrict__ x, const float* __restrict__ tu, const float* __restrict__ den,
                        const float* __restrict__ num, u16* __restrict__ out, int total) {
    int i = blockIdx.x * blockDim.x + threadIdx.x;
    if (i >= total) return;
    float z = tu[i] + num[i] / (den[i] + 1e-6f);
    z = z > 0.f ? z : 0.f;
    out[i] = f2bf(bf2f(x[i]) + z);
}

constexpr int NSB_MAX = 256;
constexpr int R_MAX = 512;
constexpr int PG = 8;

template <int NBITS>
DEV u64 match_mask(u32 key, bool valid) {
    u64 m = __ballot(valid);
#pragma unroll
    for (int b = 0; b < NBITS; ++b) {
        bool bit = (key >> b) & 1u;
        u64 bb = __ballot(bit);
        m &= bit ? bb : ~bb;
    }
    return m;
}

__global__ __launch_bounds__(512) void k_part(
    const int* __restrict__ src, const int* __restrict__ dst, const uint2* __restrict__ ea,
    const u16* __restrict__ wa, const u16* __restrict__ wb, const u16* __restrict__ wc,
    const u16* __restrict__ wu, const u16* __restrict__ wv,
    uint4* __restrict__ rec, u32* __restrict__ tab, uint4* __restrict__ wfrag, uint2* __restrict__ wcpk,
    int E, int CH, int SC, int NSB, int R, int N, u32 magSB, int SBN, u32 magNPW, int npw, int sbs
    ) {
    __shared__ __attribute__((aligned(16))) uint4 stage[4096];
    __shared__ u32 hist[8 * NSB_MAX];
    __shared__ u32 wsum[8];
    const int tid = threadIdx.x, w = tid >> 6, lane = tid & 63;
    const int p = blockIdx.x;
    STAMP(0);
    for (int i = tid; i < 8 * NSB_MAX; i += 512) hist[i] = 0u;

    const int cbase = p * CH;
    int clen = E - cbase;
    clen = clen < 0 ? 0 : (clen > CH ? CH : clen);
    const int wbeg = w * SC;
    int wend = wbeg + SC;
    wend = wend > clen ? clen : wend;

    u32 dv[PG], sv[PG];
    uint2 ev[PG];
#pragma unroll
    for (int g = 0; g < PG; ++g) {
        const int idx = wbeg + g * 64 + lane;
        if (idx < wend) {
            dv[g] = (u32)dst[cbase + idx];
            sv[g] = (u32)src[cbase + idx];
            ev[g] = ea[cbase + idx];
        } else {
            dv[g] = 0u; sv[g] = 0u; ev[g] = make_uint2(0u, 0u);
        }
    }
    STAMP(1);
    __syncthreads();

    u32 bn[PG], rk[PG], ld[PG];
    u32* myh = hist + w * NSB_MAX;
#pragma unroll
    for (int g = 0; g < PG; ++g) {
        const int idx = wbeg + g * 64 + lane;
        const bool valid = idx < wend;
        u32 b = fastdiv(dv[g], magSB);
        b = b < (u32)NSB ? b : (u32)(NSB - 1);
        const u64 m = match_mask<8>(b, valid);
        const u32 rnk = (u32)__popcll(m & ((1ull << lane) - 1ull));
        const u32 cnt = (u32)__popcll(m);
        u32 old = 0u;
        if (valid && rnk == 0u) old = atomicAdd(&myh[b], cnt);
        bn[g] = b;
        rk[g] = old;
        ld[g] = rnk | ((u32)(__ffsll((long long)m) - 1) << 8);
    }
#pragma unroll
    for (int g = 0; g < PG; ++g) rk[g] = __shfl(rk[g], (int)((ld[g] >> 8) & 63u)) + (ld[g] & 0xFFu);
    STAMP(2);
    __syncthreads();
    STAMP(3);

    u32 tot = 0u;
    if (tid < NSB) {
#pragma unroll
        for (int k = 0; k < 8; ++k) tot += hist[k * NSB_MAX + tid];
    }
    const u32 incl = wave_incl_scan(tot, lane);
    if (lane == 63) wsum[w] = incl;
    __syncthreads();
    u32 start = incl - tot;
    for (int k = 0; k < w; ++k) start += wsum[k];
    if (tid < NSB) {
        u32 run = start;
#pragma unroll
        for (int k = 0; k < 8; ++k) {
            const u32 c = hist[k * NSB_MAX + tid];
            hist[k * NSB_MAX + tid] = run;
            run += c;
        }
        tab[(size_t)tid * R + p] = (start << 16) | tot;
    }
    STAMP(4);
    __syncthreads();
    STAMP(5);

#pragma unroll
    for (int g = 0; g < PG; ++g) {
        const int idx = wbeg + g * 64 + lane;
        if (idx < wend) {
            const u32 pos = myh[bn[g]] + rk[g];
            const u32 dl = dv[g] - bn[g] * (u32)SBN;
            const u32 wv_ = fastdiv(dl, magNPW);
            const u32 loc = dl - wv_ * (u32)npw;
            const u32 sub = loc >= (u32)sbs ? 1u : 0u;
            const u32 li = loc - sub * (u32)sbs;
            const u32 s = sv[g] < (u32)N ? sv[g] : 0u;
            stage[pos] = make_uint4(s, ((wv_ * 2u + sub) << 8) | li, ev[g].x, ev[g].y);
        }
    }
    __syncthreads();
    for (int i = tid; i < clen; i += 512) rec[(size_t)cbase + i] = stage[i];

    STAMP(6);
    if (p < 4) {
        const u16* W = p == 0 ? wb : (p == 1 ? wv : (p == 2 ? wa : wu));
        const int f = tid >> 6, c = f >> 1, s = f & 1, col = lane & 15, q = lane >> 4;
        u32 v[4];
#pragma unroll
        for (int j = 0; j < 4; ++j) {
            const int k0 = 16 * q + 8 * s + 2 * j;
            v[j] = (u32)W[k0 * 64 + 16 * c + col] | ((u32)W[(k0 + 1) * 64 + 16 * c + col] << 16);
        }
        wfrag[p * 512 + tid] = make_uint4(v[0], v[1], v[2], v[3]);
    } else if (p == 4 && tid < 64) {
        const int d = tid;
        wcpk[d] = make_uint2((u32)wc[d] | ((u32)wc[64 + d] << 16), (u32)wc[128 + d] | ((u32)wc[192 + d] << 16));
    }
}

constexpr int CAPR = 4096;
constexpr int LCAP = CAPR + 16 * 32;
constexpr int STG_STRIDE = 68;
constexpr int OFF_REC = 0;
constexpr int OFF_STG = OFF_REC + LCAP * 16;
constexpr int OFF_DL = OFF_STG + 8 * 16 * STG_STRIDE * 4;
constexpr int OFF_MAP = OFF_DL + LCAP;
constexpr int OFF_PRE = OFF_MAP + CAPR * 2;
constexpr int OFF_GST = OFF_PRE + R_MAX * 4;
constexpr int OFF_FCNT = OFF_GST + R_MAX * 4;
constexpr int OFF_FTOT = OFF_FCNT + 8 * 16 * 4;
constexpr int OFF_FOFF = OFF_FTOT + 16 * 4;
constexpr int OFF_WSUM = OFF_FOFF + 16 * 4;
constexpr int OFF_WMAP = OFF_WSUM + 16 * 4;
constexpr int KM_LDS = OFF_WMAP + 8 * 512;

__global__ __launch_bounds__(512, 2) void k_main(
    const u16* __restrict__ x, const uint4* __restrict__ rec, const u32* __restrict__ tab,
    const uint4* __restrict__ wfrag, const uint2* __restrict__ wcpk, float4* __restrict__ scr,
    u16* __restrict__ out, int N, int CH, int R, int SBN, int npw, int sbs
    ) {
    __shared__ __attribute__((aligned(16))) unsigned char smem[KM_LDS];
    STAMP(0);
    uint4* srec = (uint4*)(smem + OFF_REC);
    float* stg = (float*)(smem + OFF_STG);
    u8* sdl = smem + OFF_DL;
    u16* smap = (u16*)(smem + OFF_MAP);
    u32* spre = (u32*)(smem + OFF_PRE);
    u32* sgst = (u32*)(smem + OFF_GST);
    u32* fcnt = (u32*)(smem + OFF_FCNT);
    u32* ftot = (u32*)(smem + OFF_FTOT);
    u32* foff = (u32*)(smem + OFF_FOFF);
    u32* wsum = (u32*)(smem + OFF_WSUM);

    const int sb = blockIdx.x;
    const int tid0 = threadIdx.x;

    u32 T = 0u;
    bool fast;
    u32 myoff = 0u, mytot = 0u;
    u32 mycur = 0u;
    uint4 rv[8];
    u32 rk[8];
    {
        const int tid = tid0, w = __builtin_amdgcn_readfirstlane(tid >> 6), lane = tid & 63;
        const int RPW = (R + 7) >> 3;
        const int myrun = w * RPW + lane;
        u32 dsc = 0u;
        if (lane < RPW && myrun < R) dsc = tab[(size_t)sb * R + myrun];
        if (tid < 128) fcnt[tid] = 0u;
        const u32 cn = dsc & 0xFFFFu;
        const u32 gs = (u32)myrun * (u32)CH + (dsc >> 16);
        const u32 inc = wave_incl_scan(cn, lane);
        const u32 ex = inc - cn;
        const u32 Tw = (u32)__builtin_amdgcn_readlane((int)inc, 63);
        u8* wmap = smem + OFF_WMAP + w * 512;
        const bool wok = Tw <= 512u;
        if (wok) {
            for (u32 k = 0; k < cn; ++k) wmap[ex + k] = (u8)lane;
        }
        __builtin_amdgcn_wave_barrier();
        STAMP(1);
        const u32 rel = gs - ex;
#pragma unroll
        for (int g = 0; g < 8; ++g) {
            const u32 i = g * 64 + lane;
            const bool valid = wok && i < Tw;
            const u32 run = valid ? (u32)wmap[i] : 0u;
            const u32 b = __shfl(rel, (int)run);
            rv[g] = make_uint4(0u, 0xFFu, 0u, 0u);
            if (valid) rv[g] = rec[(size_t)(b + i)];
        }
        STAMP(2);
        __syncthreads();
        u32* myf = fcnt + w * 16;
        u32 ld[8];
#pragma unroll
        for (int g = 0; g < 8; ++g) {
            const u32 i = g * 64 + lane;
            const bool valid = wok && i < Tw;
            const u32 fb = (rv[g].y >> 8) & 15u;
            const u64 m = match_mask<4>(fb, valid);
            const u32 rnk = (u32)__popcll(m & ((1ull << lane) - 1ull));
            const u32 cnt = (u32)__popcll(m);
            u32 old = 0u;
            if (valid && rnk == 0u) old = atomicAdd(&myf[fb], cnt);
            rk[g] = old;
            ld[g] = rnk | ((u32)(__ffsll((long long)m) - 1) << 8);
        }
#pragma unroll
        for (int g = 0; g < 8; ++g) rk[g] = __shfl(rk[g], (int)((ld[g] >> 8) & 63u)) + (ld[g] & 0xFFu);
        if (lane == 0) wsum[w] = wok ? Tw : 0xFFFFFFu;
        STAMP(3);
        __syncthreads();
        u32 below = 0u;
        if (lane < 16) {
#pragma unroll
            for (int k = 0; k < 8; ++k) {
                const u32 c = fcnt[k * 16 + lane];
                mytot += c;
                if (k < w) below += c;
            }
        }
#pragma unroll
        for (int k = 0; k < 8; ++k) T += wsum[k];
        fast = T <= (u32)CAPR;
        const u32 pad = (mytot + 31u) & ~31u;
        myoff = wave_incl_scan(pad, lane) - pad;
        mycur = myoff + below;
        if (lane < 16 && w == 0) { ftot[lane] = mytot; foff[lane] = myoff; }
        if (fast) {
#pragma unroll
            for (int g = 0; g < 8; ++g) {
                const u32 i = g * 64 + lane;
                const u32 fb = (rv[g].y >> 8) & 15u;
                const u32 cur = __shfl(mycur, (int)fb);
                if (i < Tw) {
                    const u32 pos = cur + rk[g];
                    srec[pos] = rv[g];
                    sdl[pos] = (u8)(rv[g].y & 0xFFu);
                }
            }
            const int b = 2 * w + (lane >> 5), k = lane & 31;
            const u32 tb_ = __shfl(mytot, b), ob_ = __shfl(myoff, b);
            if (tb_ + k < ((tb_ + 31u) & ~31u)) {
                srec[ob_ + tb_ + k] = make_uint4(0u, 0xFFu, 0u, 0u);
                sdl[ob_ + tb_ + k] = (u8)0xFFu;
            }
        }
    }

    bf16x8 WG[4][2], WH[4][2];
    {
        const int lane = tid0 & 63;
#pragma unroll
        for (int c = 0; c < 4; ++c)
#pragma unroll
            for (int s = 0; s < 2; ++s) {
                WG[c][s] = asfrag(wfrag[0 * 512 + (c * 2 + s) * 64 + lane]);
                WH[c][s] = asfrag(wfrag[1 * 512 + (c * 2 + s) * 64 + lane]);
            }
    }

    u32 gexcl = 0u, gcn = 0u;
    if (!fast) {
        const int tid = tid0, w = tid >> 6, lane = tid & 63;
        u32 st = 0u;
        if (tid < R) {
            const u32 d = tab[(size_t)sb * R + tid];
            st = d >> 16;
            gcn = d & 0xFFFFu;
        }
        const u32 incl = wave_incl_scan(gcn, lane);
        __syncthreads();
        if (lane == 63) wsum[w] = incl;
        __syncthreads();
        gexcl = incl - gcn;
        T = 0u;
#pragma unroll
        for (int k = 0; k < 8; ++k) {
            const u32 v = wsum[k];
            if (k < w) gexcl += v;
            T += v;
        }
        if (tid < R) {
            spre[tid] = gexcl;
            sgst[tid] = (u32)tid * (u32)CH + st;
        }
    }

    for (u32 rbase = 0u;; rbase += CAPR) {
        int tid = tid0;
        asm volatile("" : "+v"(tid));
        const int lane = tid & 63;
        const int w = __builtin_amdgcn_readfirstlane(tid >> 6);
        const int wnode0 = sb * SBN + w * npw;
        const u32 rc = (T - rbase) < (u32)CAPR ? (T - rbase) : (u32)CAPR;
        const bool last = rbase + CAPR >= T;
        if (!fast) {
        const u32 excl = gexcl, cn = gcn;
        if (tid < R) {
            u32 k0 = excl > rbase ? excl : rbase;
            u32 k1 = excl + cn;
            k1 = k1 < rbase + rc ? k1 : rbase + rc;
            for (u32 k = k0; k < k1; ++k) smap[k - rbase] = (u16)tid;
        }
        if (tid < 128) fcnt[tid] = 0u;
        __syncthreads();

        const u32 SC2 = (rc + 7u) >> 3;
        const u32 wb_ = (u32)w * SC2;
        u32 we_ = wb_ + SC2;
        we_ = we_ < rc ? we_ : rc;
#pragma unroll
        for (int g = 0; g < 8; ++g) {
            const u32 i = wb_ + g * 64 + lane;
            if (i < we_) {
                const u32 pp = smap[i];
                rv[g] = rec[(size_t)sgst[pp] + (rbase + i - spre[pp])];
            } else {
                rv[g] = make_uint4(0u, 0xFFu, 0u, 0u);
            }
        }
        u32* myf = fcnt + w * 16;
        u32 ld[8];
#pragma unroll
        for (int g = 0; g < 8; ++g) {
            const u32 i = wb_ + g * 64 + lane;
            const bool valid = i < we_;
            const u32 fb = (rv[g].y >> 8) & 15u;
            const u64 m = match_mask<4>(fb, valid);
            const u32 rnk = (u32)__popcll(m & ((1ull << lane) - 1ull));
            const u32 cnt = (u32)__popcll(m);
            u32 old = 0u;
            if (valid && rnk == 0u) old = atomicAdd(&myf[fb], cnt);
            rk[g] = old;
            ld[g] = rnk | ((u32)(__ffsll((long long)m) - 1) << 8);
        }
#pragma unroll
        for (int g = 0; g < 8; ++g) rk[g] = __shfl(rk[g], (int)((ld[g] >> 8) & 63u)) + (ld[g] & 0xFFu);
        __syncthreads();
        u32 below = 0u;
        mytot = 0u;
        if (lane < 16) {
#pragma unroll
            for (int k = 0; k < 8; ++k) {
                const u32 c = fcnt[k * 16 + lane];
                mytot += c;
                if (k < w) below += c;
            }
        }
        const u32 pad = (mytot + 31u) & ~31u;
        myoff = wave_incl_scan(pad, lane) - pad;
        mycur = myoff + below;
        if (lane < 16 && w == 0) { ftot[lane] = mytot; foff[lane] = myoff; }
#pragma unroll
        for (int g = 0; g < 8; ++g) {
            const u32 i = wb_ + g * 64 + lane;
            const u32 fb = (rv[g].y >> 8) & 15u;
            const u32 cur = __shfl(mycur, (int)fb);
            if (i < we_) {
                const u32 pos = cur + rk[g];
                srec[pos] = rv[g];
                sdl[pos] = (u8)(rv[g].y & 0xFFu);
            }
        }
        {
            const int b = 2 * w + (lane >> 5), k = lane & 31;
            const u32 tb_ = __shfl(mytot, b), ob_ = __shfl(myoff, b);
            if (tb_ + k < ((tb_ + 31u) & ~31u)) {
                srec[ob_ + tb_ + k] = make_uint4(0u, 0xFFu, 0u, 0u);
                sdl[ob_ + tb_ + k] = (u8)0xFFu;
            }
        }
        }
        STAMP(13);
        __syncthreads();
        STAMP(4);

#pragma unroll 1
        for (int sub = 0; sub < 2; ++sub) {
            int lane2 = lane;
            asm volatile("" : "+v"(lane2));
            const int r = lane2 & 15, q = lane2 >> 4;
            const int fb = w * 2 + sub;
            const int nb = wnode0 + sub * sbs;
            int nv = (sub == 0 ? sbs : npw - sbs);
            nv = nv < N - nb ? nv : N - nb;
            if (nv <= 0) continue;
            uint4 xo0 = make_uint4(0u, 0u, 0u, 0u), xo1 = xo0;
            if (r < nv) {
                const uint4* px = (const uint4*)(x + (size_t)(nb + r) * 64) + 2 * q;
                xo0 = px[0];
                xo1 = px[1];
            }
            bf16x8 WE[4];
#pragma unroll
            for (int c = 0; c < 4; ++c) {
                f32x4 a = {0.f, 0.f, 0.f, 0.f};
                a = MFMA16(asfrag(xo0), asfrag(wfrag[2 * 512 + (c * 2 + 0) * 64 + lane]), a);
                a = MFMA16(asfrag(xo1), asfrag(wfrag[2 * 512 + (c * 2 + 1) * 64 + lane]), a);
                uint2 wcv = make_uint2(0u, 0u);
                if (q == 3) wcv = wcpk[16 * c + r];
                WE[c] = asfrag(make_uint4(pack2(a[0], a[1]), pack2(a[2], a[3]), wcv.x, wcv.y));
            }
            f32x4 accD[4], accN[4];
            float4* sp = scr + ((size_t)(sb * 16 + fb) * 8) * 64 + lane;
            if (rbase == 0u) {
#pragma unroll
                for (int c = 0; c < 4; ++c) {
                    accD[c] = f32x4{0.f, 0.f, 0.f, 0.f};
                    accN[c] = f32x4{0.f, 0.f, 0.f, 0.f};
                }
            } else {
#pragma unroll
                for (int c = 0; c < 4; ++c) {
                    const float4 a = sp[(2 * c) * 64], b = sp[(2 * c + 1) * 64];
                    accD[c] = f32x4{a.x, a.y, a.z, a.w};
                    accN[c] = f32x4{b.x, b.y, b.z, b.w};
                }
            }
            STAMP(5 + 4 * sub);
            const u32 cnt = ftot[fb], base = foff[fb];
            const int nsteps = (int)((cnt + 31u) >> 5);
            const uint4* lrec = srec + base;
            const u8* ldl = sdl + base;
            if (nsteps > 0) {
                uint4 rc0 = lrec[r], rc1 = lrec[16 + r];
                uint4 x00, x01, x10, x11;
                {
                    const uint4* p0 = (const uint4*)(x + (size_t)rc0.x * 64) + 2 * q;
                    const uint4* p1 = (const uint4*)(x + (size_t)rc1.x * 64) + 2 * q;
                    x00 = p0[0]; x01 = p0[1]; x10 = p1[0]; x11 = p1[1];
                }
#pragma unroll 1
                for (int stp = 0; stp < nsteps; ++stp) {
                    const bool more = stp + 1 < nsteps;
                    uint4 nr0 = rc0, nr1 = rc1, n00 = x00, n01 = x01, n10 = x10, n11 = x11;
                    if (more) {
                        nr0 = lrec[(stp + 1) * 32 + r];
                        nr1 = lrec[(stp + 1) * 32 + 16 + r];
                        const uint4* p0 = (const uint4*)(x + (size_t)nr0.x * 64) + 2 * q;
                        const uint4* p1 = (const uint4*)(x + (size_t)nr1.x * 64) + 2 * q;
                        n00 = p0[0]; n01 = p0[1]; n10 = p1[0]; n11 = p1[1];
                    }
                    bf16x8 ae[2];
#pragma unroll
                    for (int t = 0; t < 2; ++t) {
                        const uint4 rcc = t == 0 ? rc0 : rc1;
                        const u32 d = rcc.y & 0xFFu;
                        const bool mt = (d >> 2) == (u32)q;
                        const u32 one = 0x3F80u << (16u * (d & 1u));
                        uint4 ax;
                        ax.x = (mt && !(d & 2u)) ? one : 0u;
                        ax.y = (mt && (d & 2u)) ? one : 0u;
                        ax.z = q == 3 ? rcc.z : 0u;
                        ax.w = q == 3 ? rcc.w : 0u;
                        ae[t] = asfrag(ax);
                    }
                    const u32 d0 = *(const u32*)(ldl + stp * 32 + 4 * q);
                    const u32 d1 = *(const u32*)(ldl + stp * 32 + 16 + 4 * q);
                    uint4 sg;
                    {
                        const u32 ur = (u32)r;
                        sg.x = (((d0 >> 0) & 0xFFu) == ur ? 0x3F80u : 0u) | (((d0 >> 8) & 0xFFu) == ur ? 0x3F800000u : 0u);
                        sg.y = (((d0 >> 16) & 0xFFu) == ur ? 0x3F80u : 0u) | (((d0 >> 24) & 0xFFu) == ur ? 0x3F800000u : 0u);
                        sg.z = (((d1 >> 0) & 0xFFu) == ur ? 0x3F80u : 0u) | (((d1 >> 8) & 0xFFu) == ur ? 0x3F800000u : 0u);
                        sg.w = (((d1 >> 16) & 0xFFu) == ur ? 0x3F80u : 0u) | (((d1 >> 24) & 0xFFu) == ur ? 0x3F800000u : 0u);
                    }
                    const bf16x8 sf = asfrag(sg);
#pragma unroll
                    for (int c = 0; c < 4; ++c) {
                        u32 ve[4], vp[4];
#pragma unroll
                        for (int t = 0; t < 2; ++t) {
                            const bf16x8 a0 = asfrag(t == 0 ? x00 : x10);
                            const bf16x8 a1 = asfrag(t == 0 ? x01 : x11);
                            f32x4 g = {0.f, 0.f, 0.f, 0.f}, h = {0.f, 0.f, 0.f, 0.f};
                            g = MFMA16(a0, WG[c][0], g);
                            g = MFMA16(a1, WG[c][1], g);
                            g = MFMA16(ae[t], WE[c], g);
                            h = MFMA16(a0, WH[c][0], h);
                            h = MFMA16(a1, WH[c][1], h);
                            float e[4], pr[4];
#pragma unroll
                            for (int i = 0; i < 4; ++i) {
                                const float tt = __builtin_amdgcn_exp2f(g[i] * -1.44269504088896341f);
                                const float et = __builtin_amdgcn_rcpf(1.0f + tt);
                                e[i] = et;
                                pr[i] = et * h[i];
                            }
                            ve[2 * t + 0] = pack2(e[0], e[1]);
                            ve[2 * t + 1] = pack2(e[2], e[3]);
                            vp[2 * t + 0] = pack2(pr[0], pr[1]);
                            vp[2 * t + 1] = pack2(pr[2], pr[3]);
                        }
                        accD[c] = MFMA16(sf, asfrag(make_uint4(ve[0], ve[1], ve[2], ve[3])), accD[c]);
                        accN[c] = MFMA16(sf, asfrag(make_uint4(vp[0], vp[1], vp[2], vp[3])), accN[c]);
                        __builtin_amdgcn_sched_barrier(0);
                    }
                    rc0 = nr0; rc1 = nr1; x00 = n00; x01 = n01; x10 = n10; x11 = n11;
                }
            }
            STAMP(6 + 4 * sub);
            if (!last) {
#pragma unroll
                for (int c = 0; c < 4; ++c) {
                    sp[(2 * c) * 64] = make_float4(accD[c][0], accD[c][1], accD[c][2], accD[c][3]);
                    sp[(2 * c + 1) * 64] = make_float4(accN[c][0], accN[c][1], accN[c][2], accN[c][3]);
                }
            } else {
                float* mystg = stg + w * 16 * STG_STRIDE;
#pragma unroll
                for (int c = 0; c < 4; ++c) {
                    f32x4 u = {0.f, 0.f, 0.f, 0.f};
                    u = MFMA16(asfrag(xo0), asfrag(wfrag[3 * 512 + (c * 2 + 0) * 64 + lane]), u);
                    u = MFMA16(asfrag(xo1), asfrag(wfrag[3 * 512 + (c * 2 + 1) * 64 + lane]), u);
#pragma unroll
                    for (int i = 0; i < 4; ++i) {
                        float z = u[i] + accN[c][i] * __builtin_amdgcn_rcpf(accD[c][i] + 1e-6f);
                        z = z > 0.f ? z : 0.f;
                        mystg[(4 * q + i) * STG_STRIDE + 16 * c + r] = z;
                    }
                }
                __builtin_amdgcn_wave_barrier();
#pragma unroll
                for (int ps = 0; ps < 2; ++ps) {
                    const int row = (lane >> 3) + 8 * ps, d8 = lane & 7;
                    if (row < nv) {
                        const size_t o = (size_t)(nb + row) * 64 + 8 * d8;
                        const uint4 xv = *(const uint4*)(x + o);
                        const float* zr = mystg + row * STG_STRIDE + 8 * d8;
                        uint4 ov;
                        ov.x = pack2(bf2f((u16)(xv.x & 0xFFFFu)) + zr[0], bf2f((u16)(xv.x >> 16)) + zr[1]);
                        ov.y = pack2(bf2f((u16)(xv.y & 0xFFFFu)) + zr[2], bf2f((u16)(xv.y >> 16)) + zr[3]);
                        ov.z = pack2(bf2f((u16)(xv.z & 0xFFFFu)) + zr[4], bf2f((u16)(xv.z >> 16)) + zr[5]);
                        ov.w = pack2(bf2f((u16)(xv.w & 0xFFFFu)) + zr[6], bf2f((u16)(xv.w >> 16)) + zr[7]);
                        *(uint4*)(out + o) = ov;
                    }
                }
                __builtin_amdgcn_wave_barrier();
            }
            STAMP(7 + 4 * sub);
        }
        if (last) break;
        __syncthreads();
    }
}

static u32 magic_for(u32 d) { return d <= 1u ? 0u : (u32)((0x100000000ull / d) + 1ull); }

extern "C" void kernel_launch(void* const* d_in, const int* in_sizes, int n_in,
                              void* d_out, int out_size, void* d_ws, size_t ws_size,
                              hipStream_t stream) {
    const u16* x = (const u16*)d_in[0];
    const u16* ea = (const u16*)d_in[1];
    const int* src = (const int*)d_in[2];
    const int* dst = (const int*)d_in[3];
    const u16* wa = (const u16*)d_in[4];
    const u16* wb = (const u16*)d_in[5];
    const u16* wc = (const u16*)d_in[6];
    const u16* wu = (const u16*)d_in[7];
    const u16* wv = (const u16*)d_in[8];
    const int N = in_sizes[0] / 64;
    const int E = in_sizes[2];

    int npw = (N + 2047) / 2048;
    npw = npw < 1 ? 1 : (npw > 32 ? 32 : npw);
    const int SBN = 8 * npw;
    const int NSB = (N + SBN - 1) / SBN;
    const int sbs = (npw + 1) / 2;
    int R = (E + 4095) / 4096;
    R = R < 256 ? 256 : R;
    const int CH = (E + R - 1) / R;
    const int SC = (CH + 7) / 8;

    const size_t rec_bytes = (size_t)R * (size_t)CH * 16;
    const size_t off_tab = (rec_bytes + 255) & ~(size_t)255;
    const size_t off_wf = (off_tab + (size_t)NSB * R * 4 + 255) & ~(size_t)255;
    const size_t off_wc = off_wf + 4 * 512 * 16;
    const size_t off_scr = (off_wc + 64 * 8 + 255) & ~(size_t)255;
    const size_t need = off_scr + (size_t)NSB * 16 * 8 * 64 * 16;
    const bool fast = N > 0 && NSB <= NSB_MAX && R <= R_MAX && CH <= 4096 && need <= ws_size;

    if (fast) {
        char* ws = (char*)d_ws;
        uint4* rec = (uint4*)ws;
        u32* tab = (u32*)(ws + off_tab);
        uint4* wfrag = (uint4*)(ws + off_wf);
        uint2* wcpk = (uint2*)(ws + off_wc);
        float4* scr = (float4*)(ws + off_scr);
        k_part<<<R, 512, 0, stream>>>(src, dst, (const uint2*)ea, wa, wb, wc, wu, wv, rec, tab, wfrag, wcpk,
                                      E, CH, SC, NSB, R, N, magic_for((u32)SBN), SBN, magic_for((u32)npw), npw, sbs
                                      );
        k_main<<<NSB, 512, 0, stream>>>(x, rec, tab, wfrag, wcpk, scr, (u16*)d_out, N, CH, R, SBN, npw, sbs
                                        );
    } else {
        float* ws = (float*)d_ws;
        const size_t NT = (size_t)N * 64;
        float* den = ws;
        float* num = ws + NT;
        float* ta = ws + 2 * NT;
        float* tb = ws + 3 * NT;
        float* th = ws + 4 * NT;
        float* tu = ws + 5 * NT;
        (void)hipMemsetAsync(den, 0, 2 * NT * sizeof(float), stream);
        g_node<<<(N + 3) / 4, 256, 0, stream>>>(x, wa, wb, wu, wv, ta, tb, th, tu, N);
        g_edge<<<(E + 3) / 4, 256, 0, stream>>>(src, dst, ea, wc, ta, tb, th, den, num, E);
        g_final<<<(int)((NT + 255) / 256), 256, 0, stream>>>(x, tu, den, num, (u16*)d_out, (int)NT);
    }
}
```
